```python
import torch

def _softmax(x, axis=-1):
    return torch.softmax(x, dim=axis)
def _gelu(x):
    return torch.nn.functional.gelu(x, approximate="tanh")
def _silu(x):
    return torch.nn.functional.silu(x)
def _sigmoid(x):
    return torch.sigmoid(x)
def _layernorm(x, axis=-1, eps=1e-5):
    m = x.mean(dim=axis, keepdim=True)
    v = x.var(dim=axis, keepdim=True, unbiased=False)
    return (x - m) / torch.sqrt(v + eps)
def _scatter_add(tgt, idx, src, axis=0):
    return tgt.clone().index_add_(axis, idx, src)
def _segment_sum(src, seg, n):
    out = torch.zeros((n,) + src.shape[1:], dtype=src.dtype)
    return out.index_add_(0, seg, src)
def _pad(x, pw):
    flat = tuple(v for pair in reversed(pw) for v in pair)
    return torch.nn.functional.pad(x, flat)

kernel_name = 'recurrent_scan__compressed_kv_cla__dense_activation_gelu__decode'

def setup_inputs():

    _w = (lambda a: (lambda t: t.to(torch.bfloat16) if t.is_floating_point() else t)(torch.as_tensor(a)))
    def _A_setup():
        B, H, Sq, Sk, D, M = 2, 32, 1, 4096, 128, 512
        Dc = 4 * D
        return {
            "x": _w((lambda *s: torch.randn(*s, dtype=torch.bfloat16) * 0.1)(B, Sq, M)),
            "c_kv": _w((lambda *s: torch.randn(*s, dtype=torch.bfloat16) * 3.2)(B, Sk, Dc)),
            "w_q": _w((lambda *s: torch.randn(*s, dtype=torch.bfloat16) * 0.8)(M, H, D) * (M ** -0.5)),
            "w_uk": _w((lambda *s: torch.randn(*s, dtype=torch.bfloat16) * 0.8)(Dc, H, D) * (Dc ** -0.5)),
            "w_uv": _w((lambda *s: torch.randn(*s, dtype=torch.bfloat16) * 3.2)(Dc, H, D) * (Dc ** -0.5)),
            "w_o": _w((lambda *s: torch.randn(*s, dtype=torch.bfloat16) * 3.2)(H, D, M) * ((H * D) ** -0.5)),
        }
    _A_in = _A_setup()

    def _B_setup():
        B, S, D, F = 2, 1, 512, 1376
        return {
            "x": _w((lambda *s: torch.randn(*s, dtype=torch.bfloat16) * 0.1)(B, S, D)),
            "w1": _w((lambda *s: torch.randn(*s, dtype=torch.bfloat16) * 3.2)(D, F) * (D ** -0.5)),
            "b1": _w((lambda *s: torch.randn(*s, dtype=torch.bfloat16) * 3.2)(F) * 0.01),
            "w2": _w((lambda *s: torch.randn(*s, dtype=torch.bfloat16) * 3.2)(F, D) * (F ** -0.5)),
            "b2": _w((lambda *s: torch.randn(*s, dtype=torch.bfloat16) * 3.2)(D) * 0.01),
        }
    _B_in = _B_setup()

    return {
        "A_x": _A_in["x"],
        "A_c_kv": _A_in["c_kv"],
        "A_w_q": _A_in["w_q"],
        "A_w_uk": _A_in["w_uk"],
        "A_w_uv": _A_in["w_uv"],
        "A_w_o": _A_in["w_o"],
        "B_x": _B_in["x"],
        "B_w1": _B_in["w1"],
        "B_b1": _B_in["b1"],
        "B_w2": _B_in["w2"],
        "B_b2": _B_in["b2"],
    }

def reference(A_x, A_c_kv, A_w_q, A_w_uk, A_w_uv, A_w_o, B_x, B_w1, B_b1, B_w2, B_b2):
    def _A_ref(x, c_kv, w_q, w_uk, w_uv, w_o):
        B, Sq, M = x.shape[0], x.shape[1], x.shape[2]
        Sk = c_kv.shape[1]
        H, D = w_q.shape[1], w_q.shape[2]
        q = torch.einsum("bqm,mhd->bhqd", x, w_q)
        k = torch.einsum("bkc,chd->bhkd", c_kv, w_uk)
        v = torch.einsum("bkc,chd->bhkd", c_kv, w_uv)
        scores = torch.einsum("bhqd,bhkd->bhqk", q, k) * (D ** -0.5)
        off = Sk - Sq
        qi = torch.arange(Sq)[:, None]
        kj = torch.arange(Sk)[None, :]
        cmask = kj <= (qi + off)
        scores = torch.where(cmask[None, None], scores, -1e9)
        p = _softmax(scores, axis=-1)
        ctx = torch.einsum("bhqk,bhkd->bhqd", p, v)
        return torch.einsum("bhqd,hdm->bqm", ctx, w_o)

    def _B_ref(x, w1, b1, w2, b2):
        h = torch.einsum("bsd,df->bsf", x, w1) + b1
        h = _gelu(h)
        return torch.einsum("bsf,fd->bsd", h, w2) + b2

    x = A_x
    xn = _layernorm(x, axis=-1)
    s = _A_ref(xn, A_c_kv, A_w_q, A_w_uk, A_w_uv, A_w_o)
    g = _silu(_B_ref(xn, B_w1, B_b1, B_w2, B_b2))
    return x + s * g
```

```cpp
#include <hip/hip_runtime.h>
#include <hip/hip_bf16.h>
#include <stdint.h>

typedef unsigned short u16;
__device__ __forceinline__ float bf2f(u16 b) { return __uint_as_float(((unsigned)b) << 16); }
__device__ __forceinline__ u16 f2bf(float f) { unsigned u = __float_as_uint(f); u += 0x7FFFu + ((u >> 16) & 1u); return (u16)(u >> 16); }
__device__ __forceinline__ float rbf(float f) { return bf2f(f2bf(f)); }

constexpr int B = 2, H = 32, SK = 4096, D = 128, M = 512, DC = 512, F = 1376;

__device__ void layernorm_block(const u16* x, float* xn, float* red) {
  const int tid = threadIdx.x;
  for (int b = 0; b < B; ++b) {
    float v0 = bf2f(x[b * M + tid]), v1 = bf2f(x[b * M + 256 + tid]);
    float s = v0 + v1;
    for (int o = 32; o >= 1; o >>= 1) s += __shfl_xor(s, o);
    __syncthreads();
    if ((tid & 63) == 0) red[tid >> 6] = s;
    __syncthreads();
    float mean = (red[0] + red[1] + red[2] + red[3]) * (1.0f / M);
    float d0 = v0 - mean, d1 = v1 - mean;
    float q = d0 * d0 + d1 * d1;
    for (int o = 32; o >= 1; o >>= 1) q += __shfl_xor(q, o);
    __syncthreads();
    if ((tid & 63) == 0) red[tid >> 6] = q;
    __syncthreads();
    float var = (red[0] + red[1] + red[2] + red[3]) * (1.0f / M);
    float mb = rbf(mean), vb = rbf(var);
    float den = rbf(sqrtf(rbf(vb + 1e-5f)));
    xn[b * M + tid] = rbf(rbf(v0 - mb) / den);
    xn[b * M + 256 + tid] = rbf(rbf(v1 - mb) / den);
  }
  __syncthreads();
}

__global__ void __launch_bounds__(256) k1(const u16* x, const u16* wq, const u16* wuk, const u16* w1, const u16* b1, const u16* w2,
                                          float* qabs, float* ypart) {
  __shared__ float xn[B * M];
  __shared__ float red[8];
  __shared__ float part[2][2][128];
  __shared__ float q[2][128];
  __shared__ float hp[8][2][32];
  const int tid = threadIdx.x;
  layernorm_block(x, xn, red);
  if (blockIdx.x < 256) {
    const int h = blockIdx.x >> 3, cs = blockIdx.x & 7;
    const int d = tid & 127, mh = tid >> 7;
    float a0 = 0.f, a1 = 0.f;
    for (int m = mh * 256; m < mh * 256 + 256; ++m) {
      float w = bf2f(wq[(size_t)m * (H * D) + h * D + d]);
      a0 += xn[m] * w; a1 += xn[M + m] * w;
    }
    part[mh][0][d] = a0; part[mh][1][d] = a1;
    __syncthreads();
    { int b = tid >> 7; q[b][d] = rbf(part[0][b][d] + part[1][b][d]); }
    __syncthreads();
    if (tid < 128) {
      int b = tid >> 6, c = cs * 64 + (tid & 63);
      float a = 0.f;
      for (int dd = 0; dd < D; ++dd) a += q[b][dd] * bf2f(wuk[(size_t)c * (H * D) + h * D + dd]);
      qabs[(b * H + h) * DC + c] = a;
    }
  } else {
    const int fs = blockIdx.x - 256;
    const int f = fs * 32 + (tid & 31), mq = tid >> 5;
    float a0 = 0.f, a1 = 0.f;
    for (int m = mq * 64; m < mq * 64 + 64; ++m) {
      float w = bf2f(w1[(size_t)m * F + f]);
      a0 += xn[m] * w; a1 += xn[M + m] * w;
    }
    hp[mq][0][tid & 31] = a0; hp[mq][1][tid & 31] = a1;
    __syncthreads();
    if (tid < 64) {
      int b = tid >> 5, ff = tid & 31;
      float s = 0.f;
      for (int i = 0; i < 8; ++i) s += hp[i][b][ff];
      float hv = rbf(rbf(s) + bf2f(b1[fs * 32 + ff]));
      float u = 0.7978845608028654f * (hv + 0.044715f * hv * hv * hv);
      float g = 0.5f * hv * (1.0f + tanhf(u));
      q[b][ff] = rbf(g);
    }
    __syncthreads();
    for (int m = tid; m < M; m += 256) {
      float a = 0.f, bb = 0.f;
      for (int ff = 0; ff < 32; ++ff) {
        float w = bf2f(w2[(size_t)(fs * 32 + ff) * M + m]);
        a += q[0][ff] * w; bb += q[1][ff] * w;
      }
      ypart[(fs * 2 + 0) * M + m] = a; ypart[(fs * 2 + 1) * M + m] = bb;
    }
  }
}

__global__ void __launch_bounds__(256) k2(const u16* ckv, const float* qabs, float* opart, float* ml) {
  __shared__ float qs[8][DC];
  __shared__ float sc[8][128];
  __shared__ float mm[8], ll[8];
  const int tid = threadIdx.x;
  const int b = blockIdx.x >> 7, hg = (blockIdx.x >> 5) & 3, ks = blockIdx.x & 31;
  for (int i = tid; i < 8 * DC; i += 256) qs[i >> 9][i & 511] = rbf(qabs[(b * H + hg * 8) * DC + i]);
  __syncthreads();
  const u16* cb = ckv + ((size_t)b * SK + ks * 128) * DC;
  {
    const int key = tid & 127, hh = tid >> 7;
    float a[4] = {0.f, 0.f, 0.f, 0.f};
    for (int c = 0; c < DC; ++c) {
      float v = bf2f(cb[(size_t)key * DC + c]);
      for (int j = 0; j < 4; ++j) a[j] += v * qs[hh * 4 + j][c];
    }
    for (int j = 0; j < 4; ++j) sc[hh * 4 + j][key] = a[j] * 0.08838834764831845f;
  }
  __syncthreads();
  if (tid < 8) {
    float m = -INFINITY;
    for (int k = 0; k < 128; ++k) m = fmaxf(m, sc[tid][k]);
    float l = 0.f;
    for (int k = 0; k < 128; ++k) { float p = __expf(sc[tid][k] - m); sc[tid][k] = p; l += p; }
    mm[tid] = m; ll[tid] = l;
  }
  __syncthreads();
  for (int c = tid; c < DC; c += 256) {
    float a[8];
    for (int j = 0; j < 8; ++j) a[j] = 0.f;
    for (int k = 0; k < 128; ++k) {
      float v = bf2f(cb[(size_t)k * DC + c]);
      for (int j = 0; j < 8; ++j) a[j] += sc[j][k] * v;
    }
    for (int j = 0; j < 8; ++j) opart[(((size_t)(b * H + hg * 8 + j)) * 32 + ks) * DC + c] = a[j];
  }
  if (tid < 8) { ml[((b * H + hg * 8 + tid) * 32 + ks) * 2] = mm[tid]; ml[((b * H + hg * 8 + tid) * 32 + ks) * 2 + 1] = ll[tid]; }
}

__global__ void __launch_bounds__(256) k3(const float* opart, const float* ml, const u16* wuv, const u16* wo, float* spart) {
  __shared__ float lat[2][DC];
  __shared__ float wgt[2][32];
  __shared__ float ctx[2][128];
  __shared__ float pp[2][2][128];
  const int tid = threadIdx.x;
  const int h = blockIdx.x >> 3, ms = blockIdx.x & 7;
  if (tid < 2) {
    const float* p = ml + ((tid * H + h) * 32) * 2;
    float m = -INFINITY;
    for (int i = 0; i < 32; ++i) m = fmaxf(m, p[2 * i]);
    float L = 0.f;
    for (int i = 0; i < 32; ++i) L += __expf(p[2 * i] - m) * p[2 * i + 1];
    for (int i = 0; i < 32; ++i) wgt[tid][i] = __expf(p[2 * i] - m) / L;
  }
  __syncthreads();
  for (int i = tid; i < 2 * DC; i += 256) {
    int b = i >> 9, c = i & 511;
    float a = 0.f;
    for (int k = 0; k < 32; ++k) a += wgt[b][k] * opart[(((size_t)(b * H + h)) * 32 + k) * DC + c];
    lat[b][c] = a;
  }
  __syncthreads();
  {
    const int d = tid & 127, ch = tid >> 7;
    float a0 = 0.f, a1 = 0.f;
    for (int c = ch * 256; c < ch * 256 + 256; ++c) {
      float w = bf2f(wuv[(size_t)c * (H * D) + h * D + d]);
      a0 += lat[0][c] * w; a1 += lat[1][c] * w;
    }
    pp[ch][0][d] = a0; pp[ch][1][d] = a1;
  }
  __syncthreads();
  { int b = tid >> 7, d = tid & 127; ctx[b][d] = rbf(pp[0][b][d] + pp[1][b][d]); }
  __syncthreads();
  if (tid < 128) {
    int b = tid >> 6, m = ms * 64 + (tid & 63);
    float a = 0.f;
    for (int d = 0; d < D; ++d) a += ctx[b][d] * bf2f(wo[((size_t)h * D + d) * M + m]);
    spart[(h * 2 + b) * M + m] = a;
  }
}

__global__ void __launch_bounds__(256) k4(const u16* x, const float* spart, const float* ypart, const u16* b2, u16* out) {
  const int i = blockIdx.x * 256 + threadIdx.x;
  const int b = i >> 9, m = i & 511;
  float s = 0.f;
  for (int h = 0; h < H; ++h) s += spart[(h * 2 + b) * M + m];
  float y = 0.f;
  for (int f = 0; f < 43; ++f) y += ypart[(f * 2 + b) * M + m];
  s = rbf(s);
  y = rbf(rbf(y) + bf2f(b2[m]));
  float g = rbf(y / (1.0f + __expf(-y)));
  out[i] = f2bf(bf2f(x[i]) + rbf(s * g));
}

extern "C" void kernel_launch(void* const* d_in, const int* in_sizes, int n_in, void* d_out, int out_size, void* d_ws, size_t ws_size,
                              hipStream_t stream) {
  const u16* x = (const u16*)d_in[0];
  const u16* ckv = (const u16*)d_in[1];
  const u16* wq = (const u16*)d_in[2];
  const u16* wuk = (const u16*)d_in[3];
  const u16* wuv = (const u16*)d_in[4];
  const u16* wo = (const u16*)d_in[5];
  const u16* w1 = (const u16*)d_in[7];
  const u16* b1 = (const u16*)d_in[8];
  const u16* w2 = (const u16*)d_in[9];
  const u16* b2 = (const u16*)d_in[10];
  float* ws = (float*)d_ws;
  float* qabs = ws;
  float* ypart = qabs + 2 * 32 * 512;
  float* ml = ypart + 44 * 2 * 512;
  float* spart = ml + 2 * 32 * 32 * 2;
  float* opart = spart + 32 * 2 * 512;
  k1<<<256 + 43, 256, 0, stream>>>(x, wq, wuk, w1, b1, w2, qabs, ypart);
  k2<<<256, 256, 0, stream>>>(ckv, qabs, opart, ml);
  k3<<<256, 256, 0, stream>>>(opart, ml, wuv, wo, spart);
  k4<<<4, 256, 0, stream>>>(x, spart, ypart, b2, (u16*)d_out);
}
```

```cpp
#include <hip/hip_runtime.h>
#include <stdint.h>

#define GAS __attribute__((address_space(1)))
#define LAS __attribute__((address_space(3)))
#define DI __device__ __forceinline__
typedef unsigned short u16;
typedef unsigned u32;
typedef unsigned long long u64;
typedef u32 v4u __attribute__((ext_vector_type(4)));
typedef u32 v2u __attribute__((ext_vector_type(2)));
typedef float f4 __attribute__((ext_vector_type(4)));
typedef float f2 __attribute__((ext_vector_type(2)));
typedef short s4 __attribute__((ext_vector_type(4)));
typedef __bf16 b8 __attribute__((ext_vector_type(8)));
typedef __bf16 b2 __attribute__((ext_vector_type(2)));

#define STAMPS 0
#define POLL_SLEEP 1
#define C_DELAY 0

constexpr int NH = 32, SK = 4096, HD = 128, MD = 512, DC = 512, FF = 1376;
constexpr int NMLP = 43;

constexpr int WS_EPOCH = 0;
constexpr int WS_DONE = 4096;
constexpr int WS_QA = 65536;
constexpr int WS_ML = WS_QA + 4 * 32768;
constexpr int WS_OB = WS_ML + 32768;
constexpr int WS_SD = WS_OB + 4 * 1024 * 1024;
constexpr int WS_END = WS_SD + (256 + 2 * NMLP) * 8192;
constexpr int WS_STAMP = 16 * 1024 * 1024;

constexpr int CROW = 1056;
constexpr int L_C = 0;
constexpr int L_Q = 128 * CROW;
constexpr int L_P = L_Q + 16384;
constexpr int L_MX = L_P + 4096;
constexpr int L_FIN = L_MX + 512;
constexpr int L_END_A = L_FIN + 512;
constexpr int L_RED = 0;
constexpr int L_XS = 8192;
constexpr int L_XP = L_XS + 4096;
constexpr int L_CX = L_XP + 2048;
constexpr int L_HR = L_CX + 8192;
constexpr int L_HX = L_HR + 2048;
constexpr int L_W1 = L_HX + 256;
constexpr int LDS_BYTES = L_END_A;
static_assert(LDS_BYTES <= 160 * 1024, "lds");

struct Params {
  const u16 *x, *ckv, *wq, *wuk, *wuv, *wo, *w1, *b1, *w2, *b2;
  u16* out;
  unsigned char* ws;
};

DI float bflo(u32 v) { return __uint_as_float(v << 16); }
DI float bfhi(u32 v) { return __uint_as_float(v & 0xFFFF0000u); }
DI u32 pk2(float lo, float hi) {
  b2 r; r[0] = (__bf16)lo; r[1] = (__bf16)hi;
  return __builtin_bit_cast(u32, r);
}
DI float rbf(float f) { return (float)(__bf16)f; }
DI f4 mfma(v4u a, v4u b, f4 c) {
  return __builtin_amdgcn_mfma_f32_16x16x32_bf16(__builtin_bit_cast(b8, a), __builtin_bit_cast(b8, b), c, 0, 0, 0);
}
template <int CTRL> DI float dpp(float x) {
  return __builtin_bit_cast(float, __builtin_amdgcn_mov_dpp(__builtin_bit_cast(int, x), CTRL, 0xf, 0xf, true));
}
constexpr int XOR1 = 0xB1, XOR2 = 0x4E, ROR4 = 0x124, ROR8 = 0x128;
DI float sum16(float x) {
  auto s = __builtin_amdgcn_permlane16_swap(__float_as_uint(x), __float_as_uint(x), false, false);
  return __uint_as_float(s[0]) + __uint_as_float(s[1]);
}
DI float sum32(float x) {
  auto s = __builtin_amdgcn_permlane32_swap(__float_as_uint(x), __float_as_uint(x), false, false);
  return __uint_as_float(s[0]) + __uint_as_float(s[1]);
}
DI float max16(float x) {
  auto s = __builtin_amdgcn_permlane16_swap(__float_as_uint(x), __float_as_uint(x), false, false);
  return fmaxf(__uint_as_float(s[0]), __uint_as_float(s[1]));
}
DI float max32(float x) {
  auto s = __builtin_amdgcn_permlane32_swap(__float_as_uint(x), __float_as_uint(x), false, false);
  return fmaxf(__uint_as_float(s[0]), __uint_as_float(s[1]));
}
DI float wave_sum(float x) {
  x += dpp<XOR1>(x); x += dpp<XOR2>(x); x += dpp<ROR4>(x); x += dpp<ROR8>(x);
  return sum32(sum16(x));
}
#define LDS_BARRIER()                                        \
  do {                                                       \
    asm volatile("s_waitcnt lgkmcnt(0)" ::: "memory");       \
    __builtin_amdgcn_s_barrier();                            \
    asm volatile("" ::: "memory");                           \
  } while (0)
#define DRAIN_VM() asm volatile("s_waitcnt vmcnt(0)" ::: "memory")

constexpr int AUX_SC1 = 16;
typedef __amdgpu_buffer_rsrc_t rsrc_t;
DI v4u ld_sc1(rsrc_t rs, int off) { return __builtin_amdgcn_raw_buffer_load_b128(rs, off, 0, AUX_SC1); }
DI void st_sc1(rsrc_t rs, int off, v4u v) { __builtin_amdgcn_raw_buffer_store_b128(v, rs, off, 0, AUX_SC1); }
#define NT 0
DI v4u ldg(const u16* p) { return NT ? __builtin_nontemporal_load((const GAS v4u*)p) : *(const GAS v4u*)p; }

constexpr u32 SPIN_CAP = 1u << 17;

#define STAMP(i)

template <int N>
DI bool precheck(rsrc_t rs, const int (&off)[N], u32 tag) {
  v4u v[N];
#pragma unroll
  for (int i = 0; i < N; ++i) v[i] = ld_sc1(rs, off[i]);
  bool any = false;
#pragma unroll
  for (int i = 0; i < N; ++i) any = any | (v[i].y == tag) | (v[i].w == tag);
  return __any(any);
}
template <int N>
DI void poll(rsrc_t rs, const int (&off)[N], u32 tag, u64 ep, int done_off, int lane, bool susp, v4u (&v)[N]) {
  u32 spins = 0;
  if (__builtin_expect(susp, 0)) {
    for (; spins < SPIN_CAP; ++spins) {
      asm volatile("" ::: "memory");
      const v4u f = ld_sc1(rs, done_off + lane * 16);
      const bool ok = (f.x == (u32)ep) & (f.y == (u32)(ep >> 32)) & (f.z == (u32)ep) & (f.w == (u32)(ep >> 32));
      if (__all(ok)) break;
      __builtin_amdgcn_s_sleep(4);
    }
  }
  for (; spins < SPIN_CAP; ++spins) {
    asm volatile("" ::: "memory");
#pragma unroll
    for (int i = 0; i < N; ++i) v[i] = ld_sc1(rs, off[i]);
    bool all = true;
#pragma unroll
    for (int i = 0; i < N; ++i) all = all & (v[i].y == tag) & (v[i].w == tag);
    if (__all(all)) break;
    __builtin_amdgcn_s_sleep(POLL_SLEEP);
  }
}
DI u64 load_epoch(unsigned char* ws) { return __hip_atomic_load((GAS u64*)(ws + WS_EPOCH), __ATOMIC_RELAXED, __HIP_MEMORY_SCOPE_AGENT); }
DI u64 next_epoch(u64 e) {
  const u32 lo = __builtin_amdgcn_readfirstlane((u32)e), hi = __builtin_amdgcn_readfirstlane((u32)(e >> 32));
  return (((u64)hi << 32) | lo) + 1ull;
}

__global__ void __launch_bounds__(512, 2) mla_decode_kernel(Params p) {
  extern __shared__ __attribute__((aligned(16))) unsigned char smem[];
  LAS unsigned char* L = (LAS unsigned char*)smem;
  const int tid = threadIdx.x, lane = tid & 63, w = __builtin_amdgcn_readfirstlane(tid >> 6);
  const int g = lane >> 4, col = lane & 15;
  const int bid = blockIdx.x;
  const rsrc_t rs = __builtin_amdgcn_make_buffer_rsrc((void*)p.ws, 0, WS_END, 0x00020000);
  STAMP(0);
  const u64 ep_raw = load_epoch(p.ws);

  if (bid < 128) {
    const int h = (bid & 7) * 4 + (bid >> 5), cq = (bid >> 3) & 3;
    const int cb = (col >> 2) & 1, cj = ((col >> 3) << 2) | (col & 3);
    const int pi = 4 * (col & 3) + (col >> 2);
    const v4u xa = ldg(p.x + 8 * lane), xb = ldg(p.x + MD + 8 * lane);
    const u32 xo = *(const GAS u32*)(p.x + (lane >> 5) * MD + 64 * w + 2 * (lane & 31));
    v4u wv[16];
    {
      const u16* src = p.wq + (size_t)(64 * w + 16 * g) * (NH * HD) + h * HD + 8 * pi;
#pragma unroll
      for (int s = 0; s < 16; ++s) wv[s] = ldg(src + (size_t)s * (NH * HD));
    }
    v4u wk[4];
    {
      const u16* src = p.wuk + (size_t)(128 * cq + 16 * w + col) * (NH * HD) + h * HD + 8 * g;
#pragma unroll
      for (int s = 0; s < 4; ++s) wk[s] = ldg(src + 32 * s);
    }
    __builtin_amdgcn_sched_barrier(0);
    const u64 ep = next_epoch(ep_raw);
    const u32 tag = (u32)ep;
    u32 mk[4];
#pragma unroll
    for (int i = 0; i < 4; ++i) mk[i] = (i == (cj >> 1)) ? ((cj & 1) ? 0xFFFF0000u : 0x0000FFFFu) : 0u;

    {
      float a[8], b[8];
#pragma unroll
      for (int i = 0; i < 4; ++i) { a[2 * i] = bflo(xa[i]); a[2 * i + 1] = bfhi(xa[i]); b[2 * i] = bflo(xb[i]); b[2 * i + 1] = bfhi(xb[i]); }
      float sa = 0.f, sb = 0.f;
#pragma unroll
      for (int i = 0; i < 8; ++i) { sa += a[i]; sb += b[i]; }
      sa = wave_sum(sa) * (1.0f / MD); sb = wave_sum(sb) * (1.0f / MD);
      float qa = 0.f, qb = 0.f;
#pragma unroll
      for (int i = 0; i < 8; ++i) { const float da = a[i] - sa, db = b[i] - sb; qa += da * da; qb += db * db; }
      qa = wave_sum(qa) * (1.0f / MD); qb = wave_sum(qb) * (1.0f / MD);
      const float mean = rbf(lane < 32 ? sa : sb), var = rbf(lane < 32 ? qa : qb);
      const float den = rbf(__builtin_sqrtf(rbf(var + 1e-5f)));
      const float n0 = rbf(bflo(xo) - mean) / den, n1 = rbf(bfhi(xo) - mean) / den;
      v2u rep; rep.x = pk2(n0, n0); rep.y = pk2(n1, n1);
      *(LAS v2u*)(L + L_XS + w * 512 + lane * 8) = rep;
      *(LAS u32*)(L + L_XP + w * 256 + lane * 4) = pk2(n0, n1);
    }
    asm volatile("s_waitcnt lgkmcnt(0)" ::: "memory");
    v4u xs[4];
#pragma unroll
    for (int i = 0; i < 4; ++i) xs[i] = *(const LAS v4u*)(L + L_XS + w * 512 + cb * 256 + g * 64 + i * 16);
    STAMP(1);
    f4 acc = {0.f, 0.f, 0.f, 0.f};
#pragma unroll
    for (int s = 0; s < 16; ++s) {
      const u32 xv = xs[s >> 2][s & 3];
      const v4u sel = {xv & mk[0], xv & mk[1], xv & mk[2], xv & mk[3]};
      acc = mfma(wv[s], sel, acc);
    }
    *(LAS f4*)(L + L_RED + w * 1024 + lane * 16) = acc;
    LDS_BARRIER();
    STAMP(2);
    f4 q = *(const LAS f4*)(L + L_RED + lane * 16);
#pragma unroll
    for (int i = 1; i < 8; ++i) q += *(const LAS f4*)(L + L_RED + i * 1024 + lane * 16);
    f4 qa4 = {0.f, 0.f, 0.f, 0.f};
#pragma unroll
    for (int s = 0; s < 4; ++s) {
      const u32 qv = pk2(q[s], q[s]);
      const v4u sel = {qv & mk[0], qv & mk[1], qv & mk[2], qv & mk[3]};
      qa4 = mfma(wk[s], sel, qa4);
    }
#pragma unroll
    for (int i = 0; i < 4; ++i) { float t = qa4[i]; t += dpp<XOR1>(t); t += dpp<XOR2>(t); t += dpp<ROR8>(t); qa4[i] = t; }
    if (cj == 0) {
      const v4u o = {pk2(qa4[0], qa4[1]), tag, pk2(qa4[2], qa4[3]), tag};
      st_sc1(rs, WS_QA + (cb * 2 + (h >> 4)) * 32768 + (4 * cq + (w >> 1)) * 2048 + (2 * (w & 1) + (g >> 1)) * 512 + (h & 15) * 32 + (g & 1) * 16, o);
    }
    STAMP(3);
    __builtin_amdgcn_sched_barrier(0);
    int offB[5];
    {
      const int ksg = lane >> 3, b = (lane >> 2) & 1;
#pragma unroll
      for (int i = 0; i < 4; ++i) offB[i] = WS_OB + ((h * 2 + b) * 32 + 4 * ksg + i) * 2048 + (32 * cq + 4 * w + (lane & 3)) * 16;
      offB[4] = WS_ML + ((h * 2 + b) * 32 + 4 * ksg + (lane & 3)) * 16;
    }
    const bool suspB = precheck<5>(rs, offB, tag);
    const int mb = w & 3, dh = w >> 2;
    const bool mlp = bid < NMLP;
    v4u w2v[4];
    if (mlp) {
      const u16* src = p.w1 + (size_t)(64 * w + (lane >> 2)) * FF + 32 * bid + 8 * (lane & 3);
#pragma unroll
      for (int i = 0; i < 4; ++i)
        __builtin_amdgcn_global_load_lds((const GAS u32*)(src + (size_t)16 * i * FF), (LAS u32*)(L + L_W1 + w * 4096 + i * 1024), 16, 0, NT ? 2 : 0);
      const u16* s2 = p.w2 + (size_t)(32 * bid + 16 * dh + 4 * g) * MD + 128 * mb + 8 * col;
#pragma unroll
      for (int s = 0; s < 4; ++s) w2v[s] = ldg(s2 + s * MD);
    }
    {
      const u16* src = p.wuv + (size_t)(128 * cq + 64 * (w >> 2) + 16 * g + 4 * (w & 3)) * (NH * HD) + h * HD + 8 * pi;
#pragma unroll
      for (int s = 0; s < 4; ++s) wk[s] = ldg(src + (size_t)s * (NH * HD));
      const u16* so = p.wo + (size_t)(h * HD + 64 * dh + 8 * g) * MD + 128 * mb + 8 * col;
#pragma unroll
      for (int s = 0; s < 16; ++s) wv[s] = ldg(so + (size_t)(32 * (s >> 3) + (s & 7)) * MD);
    }
    __builtin_amdgcn_sched_barrier(0);
    f4 yacc = {0.f, 0.f, 0.f, 0.f};
    if (mlp) {
      const float bias = bflo(p.b1[32 * bid + (tid & 31)]);
      DRAIN_VM();
      f4 h0 = {0.f, 0.f, 0.f, 0.f}, h1 = {0.f, 0.f, 0.f, 0.f};
#pragma unroll
      for (int u = 0; u < 2; ++u) {
        v4u xf = *(const LAS v4u*)(L + L_XP + w * 256 + (col & 1) * 128 + u * 64 + g * 16);
        if (col >= 2) xf = v4u{0u, 0u, 0u, 0u};
        LAS unsigned char* tb = L + L_W1 + w * 4096 + (32 * u + 8 * g + (col >> 2)) * 64 + 8 * (col & 3);
        const s4 a0 = __builtin_amdgcn_ds_read_tr16_b64_v4i16((LAS s4*)(tb)), a1 = __builtin_amdgcn_ds_read_tr16_b64_v4i16((LAS s4*)(tb + 256));
        const s4 c0 = __builtin_amdgcn_ds_read_tr16_b64_v4i16((LAS s4*)(tb + 32)), c1 = __builtin_amdgcn_ds_read_tr16_b64_v4i16((LAS s4*)(tb + 288));
        h0 = mfma(xf, __builtin_bit_cast(v4u, __builtin_shufflevector(a0, a1, 0, 1, 2, 3, 4, 5, 6, 7)), h0);
        h1 = mfma(xf, __builtin_bit_cast(v4u, __builtin_shufflevector(c0, c1, 0, 1, 2, 3, 4, 5, 6, 7)), h1);
      }
      if (lane < 16) {
        LAS float* hr = (LAS float*)(L + L_HR + w * 256);
        hr[lane] = h0[0]; hr[16 + lane] = h1[0]; hr[32 + lane] = h0[1]; hr[48 + lane] = h1[1];
      }
      LDS_BARRIER();
      if (tid < 64) {
        float s = 0.f;
#pragma unroll
        for (int i = 0; i < 8; ++i) s += *(const LAS float*)(L + L_HR + i * 256 + tid * 4);
        const float hv = rbf(rbf(s) + bias);
        const float u = 0.7978845608028654f * (hv + 0.044715f * hv * hv * hv);
        const float ge = hv / (1.0f + __expf(-2.0f * u));
        *(LAS u32*)(L + L_HX + tid * 4) = pk2(ge, ge);
      }
      LDS_BARRIER();
      const v4u hx = *(const LAS v4u*)(L + L_HX + cb * 128 + dh * 64 + g * 16);
#pragma unroll
      for (int s = 0; s < 4; ++s) {
        const u32 xv = hx[s];
        const v4u sel = {xv & mk[0], xv & mk[1], xv & mk[2], xv & mk[3]};
        yacc = mfma(w2v[s], sel, yacc);
      }
    }
    DRAIN_VM();
    LDS_BARRIER();
    if (tid == 0) __hip_atomic_store((GAS u64*)(p.ws + WS_DONE) + bid, ep, __ATOMIC_RELAXED, __HIP_MEMORY_SCOPE_AGENT);
    STAMP(4);
    v4u gv[5];
    poll<5>(rs, offB, tag, ep, WS_DONE + 1024, lane, suspB, gv);
    STAMP(5);
    {
      const float m = __uint_as_float(gv[4].x), l = __uint_as_float(gv[4].z);
      float M = fmaxf(m, dpp<XOR1>(m)); M = fmaxf(M, dpp<XOR2>(M)); M = fmaxf(M, dpp<ROR8>(M)); M = max32(max16(M));
      const float e = __expf(m - M);
      float Ls = e * l;
      Ls += dpp<XOR1>(Ls); Ls += dpp<XOR2>(Ls); Ls += dpp<ROR8>(Ls); Ls = sum32(sum16(Ls));
      const float e0 = dpp<0x00>(e), e1 = dpp<0x55>(e), e2 = dpp<0xAA>(e), e3 = dpp<0xFF>(e);
      float r0 = e0 * bflo(gv[0].x), r1 = e0 * bfhi(gv[0].x), r2 = e0 * bflo(gv[0].z), r3 = e0 * bfhi(gv[0].z);
      r0 += e1 * bflo(gv[1].x); r1 += e1 * bfhi(gv[1].x); r2 += e1 * bflo(gv[1].z); r3 += e1 * bfhi(gv[1].z);
      r0 += e2 * bflo(gv[2].x); r1 += e2 * bfhi(gv[2].x); r2 += e2 * bflo(gv[2].z); r3 += e2 * bfhi(gv[2].z);
      r0 += e3 * bflo(gv[3].x); r1 += e3 * bfhi(gv[3].x); r2 += e3 * bflo(gv[3].z); r3 += e3 * bfhi(gv[3].z);
      auto s0 = __builtin_amdgcn_permlane32_swap(__float_as_uint(r0), __float_as_uint(r2), false, false);
      auto s1 = __builtin_amdgcn_permlane32_swap(__float_as_uint(r1), __float_as_uint(r3), false, false);
      const float u0 = __uint_as_float(s0[0]) + __uint_as_float(s0[1]), u1 = __uint_as_float(s1[0]) + __uint_as_float(s1[1]);
      auto s2 = __builtin_amdgcn_permlane16_swap(__float_as_uint(u0), __float_as_uint(u1), false, false);
      float v = __uint_as_float(s2[0]) + __uint_as_float(s2[1]);
      v += dpp<ROR8>(v);
      v = v / Ls;
      const u32 vr = pk2(v, v);
      const u32 c0 = __builtin_amdgcn_mov_dpp(vr, 0x00, 0xf, 0xf, true), c1 = __builtin_amdgcn_mov_dpp(vr, 0x55, 0xf, 0xf, true);
      const u32 c2 = __builtin_amdgcn_mov_dpp(vr, 0xAA, 0xf, 0xf, true), c3 = __builtin_amdgcn_mov_dpp(vr, 0xFF, 0xf, 0xf, true);
      f4 ca = {0.f, 0.f, 0.f, 0.f};
      ca = mfma(wk[0], v4u{c0 & mk[0], c0 & mk[1], c0 & mk[2], c0 & mk[3]}, ca);
      ca = mfma(wk[1], v4u{c1 & mk[0], c1 & mk[1], c1 & mk[2], c1 & mk[3]}, ca);
      ca = mfma(wk[2], v4u{c2 & mk[0], c2 & mk[1], c2 & mk[2], c2 & mk[3]}, ca);
      ca = mfma(wk[3], v4u{c3 & mk[0], c3 & mk[1], c3 & mk[2], c3 & mk[3]}, ca);
      *(LAS f4*)(L + L_RED + w * 1024 + lane * 16) = ca;
    }
    LDS_BARRIER();
    {
      f2 c = *(const LAS f2*)(L + L_RED + lane * 16 + dh * 8);
#pragma unroll
      for (int i = 1; i < 8; ++i) c += *(const LAS f2*)(L + L_RED + i * 1024 + lane * 16 + dh * 8);
      LAS u32* cx = (LAS u32*)(L + L_CX + w * 1024);
      cx[cb * 64 + 8 * g + cj] = pk2(c[0], c[0]);
      cx[cb * 64 + 32 + 8 * g + cj] = pk2(c[1], c[1]);
      asm volatile("s_waitcnt lgkmcnt(0)" ::: "memory");
      v4u cs[4];
#pragma unroll
      for (int i = 0; i < 4; ++i) cs[i] = *(const LAS v4u*)(L + L_CX + w * 1024 + cb * 256 + (i >> 1) * 128 + g * 32 + (i & 1) * 16);
      f4 sa = {0.f, 0.f, 0.f, 0.f};
#pragma unroll
      for (int s = 0; s < 16; ++s) {
        const u32 xv = cs[s >> 2][s & 3];
        const v4u sel = {xv & mk[0], xv & mk[1], xv & mk[2], xv & mk[3]};
        sa = mfma(wv[s], sel, sa);
      }
      const int base = WS_SD + (bid * 2 + dh) * 8192 + mb * 2048 + lane * 16;
      st_sc1(rs, base, v4u{__float_as_uint(sa[0]), tag, __float_as_uint(sa[1]), tag});
      st_sc1(rs, base + 1024, v4u{__float_as_uint(sa[2]), tag, __float_as_uint(sa[3]), tag});
      if (mlp) {
        const int by = base + 256 * 8192;
        st_sc1(rs, by, v4u{__float_as_uint(yacc[0]), tag, __float_as_uint(yacc[1]), tag});
        st_sc1(rs, by + 1024, v4u{__float_as_uint(yacc[2]), tag, __float_as_uint(yacc[3]), tag});
      }
    }
    STAMP(6);
    DRAIN_VM();
    LDS_BARRIER();
    if (tid == 0) __hip_atomic_store((GAS u64*)(p.ws + WS_DONE + 2048) + bid, ep, __ATOMIC_RELAXED, __HIP_MEMORY_SCOPE_AGENT);
    STAMP(7);
  } else {
    const int a = bid - 128, hg = (a >> 3) & 1, chunk = (a & 7) * 8 + (a >> 4), b = chunk >> 5, ks = chunk & 31;
    const u64 ep = next_epoch(ep_raw);
    const u32 tag = (u32)ep;
    int offA[4];
#pragma unroll
    for (int i = 0; i < 4; ++i) offA[i] = WS_QA + (b * 2 + hg) * 32768 + w * 4096 + i * 1024 + lane * 16;
    const bool suspA = precheck<4>(rs, offA, tag);
    __builtin_amdgcn_sched_barrier(0);
    {
      const u16* src = p.ckv + ((size_t)chunk * 128 + 16 * w) * DC + 8 * lane;
#pragma unroll
      for (int i = 0; i < 16; ++i)
        __builtin_amdgcn_global_load_lds((const GAS u32*)(src + i * DC), (LAS u32*)(L + L_C + (16 * w + i) * CROW), 16, 0, NT ? 2 : 0);
    }
    const int fb = a & 1, fm = 128 * (a >> 5) + 8 * (4 * ((a >> 2) & 3) + 2 * ((a >> 4) & 1) + ((lane >> 2) & 1)) + 4 * ((a >> 1) & 1) + (lane & 3);
    const float fx = bflo(p.x[fb * MD + fm]), fb2 = bflo(p.b2[fm]);
    DRAIN_VM();
    LDS_BARRIER();
    STAMP(1);
    v4u cr[16];
#pragma unroll
    for (int s = 0; s < 16; ++s) cr[s] = *(const LAS v4u*)(L + L_C + (16 * w + col) * CROW + s * 64 + g * 16);
    v4u ct[16];
    {
      LAS unsigned char* tb = L + L_C + (4 * g + (col >> 2)) * CROW + 128 * w + 8 * (col & 3);
#pragma unroll
      for (int t = 0; t < 4; ++t)
#pragma unroll
        for (int u = 0; u < 4; ++u) {
          const s4 lo = __builtin_amdgcn_ds_read_tr16_b64_v4i16((LAS s4*)(tb + (32 * u) * CROW + 32 * t));
          const s4 hi = __builtin_amdgcn_ds_read_tr16_b64_v4i16((LAS s4*)(tb + (32 * u + 16) * CROW + 32 * t));
          ct[t * 4 + u] = __builtin_bit_cast(v4u, __builtin_shufflevector(lo, hi, 0, 1, 2, 3, 4, 5, 6, 7));
        }
    }
    __builtin_amdgcn_sched_barrier(0);
    STAMP(2);
    {
      v4u gv[4];
      poll<4>(rs, offA, tag, ep, WS_DONE, lane, suspA, gv);
#pragma unroll
      for (int i = 0; i < 4; ++i) *(LAS v2u*)(L + L_Q + w * 2048 + i * 512 + lane * 8) = v2u{gv[i].x, gv[i].z};
    }
    LDS_BARRIER();
    STAMP(3);
    f4 sc = {0.f, 0.f, 0.f, 0.f};
#pragma unroll
    for (int s = 0; s < 16; ++s) sc = mfma(cr[s], *(const LAS v4u*)(L + L_Q + s * 1024 + lane * 16), sc);
#pragma unroll
    for (int r = 0; r < 4; ++r) sc[r] = rbf(rbf(sc[r]) * 0.08838834764831845f);
    float mx = fmaxf(fmaxf(sc[0], sc[1]), fmaxf(sc[2], sc[3]));
    mx = max32(max16(mx));
    if (lane < 16) *(LAS float*)(L + L_MX + lane * 32 + w * 4) = mx;
    LDS_BARRIER();
    {
      const f4 m0 = *(const LAS f4*)(L + L_MX + col * 32), m1 = *(const LAS f4*)(L + L_MX + col * 32 + 16);
      mx = fmaxf(fmaxf(fmaxf(m0[0], m0[1]), fmaxf(m0[2], m0[3])), fmaxf(fmaxf(m1[0], m1[1]), fmaxf(m1[2], m1[3])));
    }
    {
      const float p0 = __expf(sc[0] - mx), p1 = __expf(sc[1] - mx), p2 = __expf(sc[2] - mx), p3 = __expf(sc[3] - mx);
      *(LAS v2u*)(L + L_P + (w >> 1) * 1024 + lane * 16 + (w & 1) * 8) = v2u{pk2(p0, p1), pk2(p2, p3)};
    }
    LDS_BARRIER();
    STAMP(4);
    v4u pf[4];
#pragma unroll
    for (int u = 0; u < 4; ++u) pf[u] = *(const LAS v4u*)(L + L_P + u * 1024 + lane * 16);
    f4 o[4];
#pragma unroll
    for (int t = 0; t < 4; ++t) {
      o[t] = f4{0.f, 0.f, 0.f, 0.f};
#pragma unroll
      for (int u = 0; u < 4; ++u) o[t] = mfma(pf[u], ct[t * 4 + u], o[t]);
    }
    {
      const int base = WS_OB + (((16 * hg + 4 * g) * 2 + b) * 32 + ks) * 2048 + (16 * w + col) * 16;
#pragma unroll
      for (int r = 0; r < 4; ++r) st_sc1(rs, base + r * (2 * 32 * 2048), v4u{pk2(o[0][r], o[1][r]), tag, pk2(o[2][r], o[3][r]), tag});
    }
    if (w == 0) {
      float l = 0.f;
      const b2 one = {(__bf16)1.0f, (__bf16)1.0f};
#pragma unroll
      for (int u = 0; u < 4; ++u)
#pragma unroll
        for (int i = 0; i < 4; ++i) { const u32 pp = pf[u][i]; l = __builtin_amdgcn_fdot2_f32_bf16(__builtin_bit_cast(b2, pp), one, l, false); }
      l = sum32(sum16(l));
      if (lane < 16) st_sc1(rs, WS_ML + (((16 * hg + lane) * 2 + b) * 32 + ks) * 16, v4u{__float_as_uint(mx), tag, __float_as_uint(l), tag});
    }
    STAMP(5);
    DRAIN_VM();
    LDS_BARRIER();
    if (tid == 0) __hip_atomic_store((GAS u64*)(p.ws + WS_DONE + 1024) + a, ep, __ATOMIC_RELAXED, __HIP_MEMORY_SCOPE_AGENT);
    {
      v4u gv[3];
      int off[3];
      const int slot = tid >> 2;
      const bool hasy = slot < 2 * NMLP;
      off[0] = WS_SD + slot * 8192 + a * 64 + (lane & 3) * 16;
      off[1] = off[0] + 128 * 8192;
      off[2] = WS_SD + (256 + (hasy ? slot : 0)) * 8192 + a * 64 + (lane & 3) * 16;
      const bool suspD = precheck<3>(rs, off, tag);
      poll<3>(rs, off, tag, ep, WS_DONE + 2048, lane, suspD, gv);
      STAMP(6);
      f4 t;
      t[0] = __uint_as_float(gv[0].x) + __uint_as_float(gv[1].x);
      t[1] = __uint_as_float(gv[0].z) + __uint_as_float(gv[1].z);
      t[2] = hasy ? __uint_as_float(gv[2].x) : 0.f;
      t[3] = hasy ? __uint_as_float(gv[2].z) : 0.f;
#pragma unroll
      for (int i = 0; i < 4; ++i) { float v = t[i]; v += dpp<ROR4>(v); v += dpp<ROR8>(v); t[i] = sum32(sum16(v)); }
      if (lane < 4) *(LAS f4*)(L + L_FIN + w * 64 + lane * 16) = t;
    }
    LDS_BARRIER();
    if (tid < 8) {
      float s = 0.f, y = 0.f;
#pragma unroll
      for (int i = 0; i < 8; ++i) {
        s += *(const LAS float*)(L + L_FIN + i * 64 + (lane & 3) * 16 + (lane >> 2) * 4);
        y += *(const LAS float*)(L + L_FIN + i * 64 + (lane & 3) * 16 + (lane >> 2) * 4 + 8);
      }
      s = rbf(s);
      y = rbf(rbf(y) + fb2);
      const float gt = rbf(y / (1.0f + __expf(-y)));
      const float r = fx + rbf(s * gt);
      p.out[fb * MD + fm] = __builtin_bit_cast(u16, (__bf16)r);
      if (a == 0 && tid == 0) __hip_atomic_store((GAS u64*)(p.ws + WS_EPOCH), ep, __ATOMIC_RELAXED, __HIP_MEMORY_SCOPE_AGENT);
    }
    STAMP(7);
  }
}

extern "C" void kernel_launch(void* const* d_in, const int* in_sizes, int n_in, void* d_out, int out_size, void* d_ws, size_t ws_size,
                              hipStream_t stream) {
  Params p;
  p.x = (const u16*)d_in[0];
  p.ckv = (const u16*)d_in[1];
  p.wq = (const u16*)d_in[2];
  p.wuk = (const u16*)d_in[3];
  p.wuv = (const u16*)d_in[4];
  p.wo = (const u16*)d_in[5];
  p.w1 = (const u16*)d_in[7];
  p.b1 = (const u16*)d_in[8];
  p.w2 = (const u16*)d_in[9];
  p.b2 = (const u16*)d_in[10];
  p.out = (u16*)d_out;
  p.ws = (unsigned char*)d_ws;
  (void)hipFuncSetAttribute((const void*)mla_decode_kernel, hipFuncAttributeMaxDynamicSharedMemorySize, LDS_BYTES);
  hipLaunchKernelGGL(mla_decode_kernel, dim3(256), dim3(512), LDS_BYTES, stream, p);
}
```

```cpp
#define X 1
#include <hip/hip_runtime.h>
#include <stdint.h>

#define GAS __attribute__((address_space(1)))
#define LAS __attribute__((address_space(3)))
#define DI __device__ __forceinline__
typedef unsigned short u16;
typedef unsigned u32;
typedef unsigned long long u64;
typedef u32 v4u __attribute__((ext_vector_type(4)));
typedef u32 v2u __attribute__((ext_vector_type(2)));
typedef float f4 __attribute__((ext_vector_type(4)));
typedef float f2 __attribute__((ext_vector_type(2)));
typedef short s4 __attribute__((ext_vector_type(4)));
typedef __bf16 b8 __attribute__((ext_vector_type(8)));
typedef __bf16 b2 __attribute__((ext_vector_type(2)));

#define STAMPS 0
#define STAMP_WAVE 0
#define HELP_UK 1
#define TLB_TOUCH 1
#define CODE_TOUCH 12544
#define PF2_TOUCH 0
#define PRE_QA 1
#define PRE_SB 2
#define HELP_DELAY 20
#define TUNE 0
#define UV_EARLY 0
#define MLP_B1 2
#define C_DELAY 0
#define NT_ONCE 0
#define NT_SHARED 0
#define SENT_A_WAVE 2
#define SENT_A_STEP 15
#define GSLP 8
#define ASLP 2
#define DSLP 2
#define SSLP 2
#define DLY_A 5
#define DLY_B 1
#define SENT_B_POS 1
#define DLY_D 14
#define PF2_DELAY 0

constexpr int NH = 32, SK = 4096, HD = 128, MD = 512, DC = 512, FF = 1376;
constexpr int NMLP = 43;

constexpr int WS_EPOCH = 0;
constexpr int WS_SA = 4096;
constexpr int WS_SDS = WS_SA + 32768;
constexpr int WS_ML = WS_SDS + 32768;
constexpr int WS_SB = WS_ML + 32768;
constexpr int WS_QA = 262144;
constexpr int WS_OB = WS_QA + 4 * 32768;
constexpr int WS_SD = WS_OB + 4 * 1024 * 1024;
constexpr int WS_END = WS_SD + (128 + NMLP) * 4096;
constexpr int WS_STAMP = 16 * 1024 * 1024;

constexpr int CROW = 1056;
constexpr int L_C = 0;
constexpr int L_F = 128 * CROW;
constexpr int L_Q = L_F;
constexpr int L_P = L_Q + 16 * CROW;
constexpr int L_MX = L_P + 4096;
constexpr int L_FIN = L_Q;
constexpr int L_XP = L_MX + 512;
constexpr int L_HR = L_XP + 2048;
constexpr int L_HX = L_HR + 2048;
constexpr int L_W1 = 65536;
constexpr int L_XS = 0;
constexpr int L_UK = 4096;
constexpr int L_RED = L_F;
constexpr int L_UV = L_RED + 8192;
constexpr int L_QST = L_UV;
constexpr int L_FLAG = L_UV + 512;
constexpr int L_LAT = L_UV + 16384;
constexpr int L_CTX = L_LAT + 1024;
constexpr int LDS_BYTES = L_F + 27 * 1024;
static_assert(L_HX + 128 <= LDS_BYTES && L_CTX + 2048 <= LDS_BYTES && LDS_BYTES <= 160 * 1024, "lds");

struct Params {
  const u16 *x, *wq, *wuk;
  unsigned char* ws;
  const u16 *ckv, *wuv, *wo, *b2;
  const u16 *w1, *b1, *w2;
  u16* out;
};

DI float bflo(u32 v) { return __uint_as_float(v << 16); }
DI float bfhi(u32 v) { return __uint_as_float(v & 0xFFFF0000u); }
DI u32 pk2(float lo, float hi) {
  b2 r; r[0] = (__bf16)lo; r[1] = (__bf16)hi;
  return __builtin_bit_cast(u32, r);
}
DI float rbf(float f) { return (float)(__bf16)f; }
DI f4 mfma(v4u a, v4u b, f4 c) {
  return __builtin_amdgcn_mfma_f32_16x16x32_bf16(__builtin_bit_cast(b8, a), __builtin_bit_cast(b8, b), c, 0, 0, 0);
}
template <int CTRL> DI float dpp(float x) {
  return __builtin_bit_cast(float, __builtin_amdgcn_mov_dpp(__builtin_bit_cast(int, x), CTRL, 0xf, 0xf, true));
}
constexpr int XOR1 = 0xB1, XOR2 = 0x4E, ROR4 = 0x124, ROR8 = 0x128, HMIR = 0x141;
DI float swap16_add(float a, float b) {
  auto s = __builtin_amdgcn_permlane16_swap(__float_as_uint(a), __float_as_uint(b), false, false);
  return __uint_as_float(s[0]) + __uint_as_float(s[1]);
}
DI float swap32_add(float a, float b) {
  auto s = __builtin_amdgcn_permlane32_swap(__float_as_uint(a), __float_as_uint(b), false, false);
  return __uint_as_float(s[0]) + __uint_as_float(s[1]);
}
DI float sum16(float x) { return swap16_add(x, x); }
DI float sum32(float x) { return swap32_add(x, x); }
DI float max16(float x) {
  auto s = __builtin_amdgcn_permlane16_swap(__float_as_uint(x), __float_as_uint(x), false, false);
  return fmaxf(__uint_as_float(s[0]), __uint_as_float(s[1]));
}
DI float max32(float x) {
  auto s = __builtin_amdgcn_permlane32_swap(__float_as_uint(x), __float_as_uint(x), false, false);
  return fmaxf(__uint_as_float(s[0]), __uint_as_float(s[1]));
}
DI float wave_sum(float x) {
  x += dpp<XOR1>(x); x += dpp<XOR2>(x); x += dpp<ROR4>(x); x += dpp<ROR8>(x);
  return sum32(sum16(x));
}
#define LDS_BARRIER()                                        \
  do {                                                       \
    asm volatile("s_waitcnt lgkmcnt(0)" ::: "memory");       \
    __builtin_amdgcn_s_barrier();                            \
    asm volatile("" ::: "memory");                           \
  } while (0)
#define DRAIN_VM() asm volatile("s_waitcnt vmcnt(0)" ::: "memory")

constexpr int AUX_SC1 = 16;
typedef __amdgpu_buffer_rsrc_t rsrc_t;
DI v4u ld_sc1(rsrc_t rs, int off) { return __builtin_amdgcn_raw_buffer_load_b128(rs, off, 0, AUX_SC1); }
DI void st_sc1(rsrc_t rs, int off, v4u v) { __builtin_amdgcn_raw_buffer_store_b128(v, rs, off, 0, AUX_SC1); }
DI rsrc_t mkrs(const void* p, int bytes) { return __builtin_amdgcn_make_buffer_rsrc((void*)p, 0, bytes, 0x00020000); }
template <int NT> DI v4u ldb(rsrc_t rs, int voff, int soff) { return __builtin_amdgcn_raw_buffer_load_b128(rs, voff, soff, NT ? 2 : 0); }
template <int NT> DI void dma(rsrc_t rs, LAS unsigned char* dst, int voff, int soff) {
  __builtin_amdgcn_raw_ptr_buffer_load_lds(rs, (LAS void*)dst, 16, voff, soff, 0, NT ? 2 : 0);
}
DI v4u trpair(LAS unsigned char* lo, LAS unsigned char* hi) {
  const s4 a = __builtin_amdgcn_ds_read_tr16_b64_v4i16((LAS s4*)lo), b = __builtin_amdgcn_ds_read_tr16_b64_v4i16((LAS s4*)hi);
  return __builtin_bit_cast(v4u, __builtin_shufflevector(a, b, 0, 1, 2, 3, 4, 5, 6, 7));
}
DI u16 bf16bits(float f) { return __builtin_bit_cast(u16, (__bf16)f); }
DI void layer_norm8(v4u x, float (&n)[8]) {
  float a[8];
#pragma unroll
  for (int i = 0; i < 4; ++i) { a[2 * i] = bflo(x[i]); a[2 * i + 1] = bfhi(x[i]); }
  const float s = wave_sum(((a[0] + a[1]) + (a[2] + a[3])) + ((a[4] + a[5]) + (a[6] + a[7]))) * (1.0f / MD);
  float q0 = 0.f, q1 = 0.f;
#pragma unroll
  for (int i = 0; i < 8; i += 2) { const float d0 = a[i] - s, d1 = a[i + 1] - s; q0 += d0 * d0; q1 += d1 * d1; }
  const float q = wave_sum(q0 + q1) * (1.0f / MD);
  const float mean = rbf(s);
  const float den = rbf(__builtin_sqrtf(rbf(rbf(q) + 1e-5f)));
  const float r = 1.0f / den;
#pragma unroll
  for (int i = 0; i < 8; ++i) {
    const float d = rbf(a[i] - mean), t = d * r;
    n[i] = __builtin_fmaf(__builtin_fmaf(-t, den, d), r, t);
  }
}
DI v4u sel4(u32 v, const u32 (&mk)[4]) { return v4u{v & mk[0], v & mk[1], v & mk[2], v & mk[3]}; }

constexpr u32 SPIN_CAP = 1u << 17;

#define STAMP(i)
#define STAMP_FLUSH()

#define DELAY(i, d) do { if (d) __builtin_amdgcn_s_sleep(d); } while (0)
template <int N, int SLP>
DI void poll(rsrc_t rs, const int (&off)[N], u32 tag, v4u (&v)[N]) {
#pragma unroll 1
  for (u32 spins = 0; spins < SPIN_CAP; ++spins) {
    asm volatile("" ::: "memory");
#pragma unroll
    for (int i = 0; i < N; ++i) v[i] = ld_sc1(rs, off[i]);
    bool all = true;
#pragma unroll
    for (int i = 0; i < N; ++i) all = all & (v[i].y == tag) & (v[i].w == tag);
    if (__all(all)) break;
    if (SLP) __builtin_amdgcn_s_sleep(SLP);
  }
}

__global__ void __launch_bounds__(512, 2) mla_decode_kernel(Params p) {
  extern __shared__ __attribute__((aligned(16))) unsigned char smem[];
  LAS unsigned char* L = (LAS unsigned char*)smem;
  const int tid = threadIdx.x, lane = tid & 63, w = __builtin_amdgcn_readfirstlane(tid >> 6);
  const int g = lane >> 4, col = lane & 15;
  const int bid = blockIdx.x;
  const rsrc_t rs = mkrs(p.ws, WS_END);
  STAMP(0);
  LAS unsigned char* const tb = L + L_C + (4 * g + (col >> 2)) * CROW + 128 * w + 8 * (col & 3);

  const bool isW = bid < 128;
  const int h = (bid & 7) * 4 + ((bid >> 5) & 3), cq = (bid >> 3) & 3;
  v4u x0;
  if (w >= 6) x0 = ldb<0>(mkrs(p.x, 2 * MD * 2), lane * 16, (w & 1) * (MD * 2));
  v4u wv[16];
  u32 tlbv = 0;
  const rsrc_t rq = mkrs(isW ? p.wq : p.wuk, MD * NH * HD * 2);
  const int vq = isW ? 16 * g * 8192 + col * 16 : lane * 16;
  const int sq = 64 * w * 8192 + h * 256;
  {
    if (!isW && w < 2) {
      const int j = ((bid >> 3) & 15) + 16 * w;
      const u64 tp = w == 0 ? 0x5554333222111130ull : 0x00000000B9A87666ull, to = w == 0 ? 0x2100210210210000ull : 0x0000000000000210ull;
      const int pi = (int)(tp >> (4 * (j & 15))) & 15, oi = (int)(to >> (4 * (j & 15))) & 3;
      const unsigned char* base = (const unsigned char*)((const __attribute__((address_space(4))) u64*)__builtin_amdgcn_kernarg_segment_ptr())[pi];
      tlbv = *(const GAS u32*)(base + (pi == 4 ? (size_t)(bid & 7) * 1048576 : (oi == 0 ? 0 : oi == 1 ? 2097152 : 4194304 - 128)) + (pi == 3 ? 2048 : 0));
    }
    if (!isW && w == 2 && lane < 8) {
      const u64 pc = __builtin_amdgcn_s_getpc() & ~127ull;
      tlbv = *(const GAS u32*)(pc + min((((bid >> 3) & 15) * 8 + lane) * 128, CODE_TOUCH));
    }
    if (!isW) __builtin_amdgcn_s_sleep(HELP_DELAY);
    const int sf = isW ? sq + 4 * cq * 8192 : (32 * ((bid >> 3) & 15) + 4 * w) * 8192 + (bid & 7) * 1024;
#pragma unroll
    for (int s = 0; s < 4; ++s) wv[s] = ldb<NT_SHARED>(rq, vq, sf + s * 8192);
  }
  const u32 ep_raw = __hip_atomic_load((GAS u32*)(p.ws + WS_EPOCH), __ATOMIC_RELAXED, __HIP_MEMORY_SCOPE_AGENT);
  __builtin_amdgcn_sched_barrier(0);

  if (__builtin_expect(isW, 1)) {
    const int cb = (col >> 2) & 1, cj = ((col >> 3) << 2) | (col & 3);
    const rsrc_t rk = mkrs(p.wuk, DC * NH * HD * 2);
    const int ukr = col;
    v4u wk[4];
    u32 mk[4];
#pragma unroll
    for (int i = 0; i < 4; ++i) mk[i] = (i == (cj >> 1)) ? ((cj & 1) ? 0xFFFF0000u : 0x0000FFFFu) : 0u;
    STAMP(10);
    if (tid == 0) *(LAS v2u*)(L + L_FLAG) = v2u{0u, 0u};
    LDS_BARRIER();
    if (w >= 6) {
      float n[8];
      layer_norm8(x0, n);
      *(LAS v4u*)(L + L_XS + (w & 1) * 2048 + lane * 32) = v4u{pk2(n[0], n[0]), pk2(n[1], n[1]), pk2(n[2], n[2]), pk2(n[3], n[3])};
      *(LAS v4u*)(L + L_XS + (w & 1) * 2048 + lane * 32 + 16) = v4u{pk2(n[4], n[4]), pk2(n[5], n[5]), pk2(n[6], n[6]), pk2(n[7], n[7])};
      asm volatile("s_waitcnt lgkmcnt(0)" ::: "memory");
      *(volatile LAS u32*)(L + L_FLAG + (w & 1) * 4) = 1u;
    }
    __builtin_amdgcn_sched_barrier(0);
    STAMP(11);
#pragma unroll
    for (int s = 4; s < 16; ++s) wv[s] = ldb<NT_SHARED>(rq, vq, sq + (4 * ((cq + (s >> 2)) & 3) + (s & 3)) * 8192);
#pragma unroll
    for (int s = 0; s < 4; ++s) wk[s] = ldb<NT_ONCE>(rk, g * 8192 + col * 16, (128 * cq + 16 * w + 4 * s) * 8192 + h * 256);
    const int dh = w & 1, kq = w >> 1;
    v4u wu[4];
    const rsrc_t rv = mkrs(p.wuv, DC * NH * HD * 2), ro = mkrs(p.wo, NH * HD * MD * 2);
    u32 tch[4] = {0u, 0u, 0u, 0u};
    __builtin_amdgcn_sched_barrier(0);
    STAMP(1);
    for (u32 spins = 0; spins < SPIN_CAP; ++spins) {
      const v2u f = *(volatile LAS v2u*)(L + L_FLAG);
      if (f.x & f.y) break;
      __builtin_amdgcn_s_sleep(1);
    }
    asm volatile("" ::: "memory");
    v4u sel[16];
    {
      v4u xs[4];
#pragma unroll
      for (int i = 0; i < 4; ++i) xs[i] = *(const LAS v4u*)(L + L_XS + cb * 2048 + w * 256 + g * 64 + ((cq + i) & 3) * 16);
#pragma unroll
      for (int s = 0; s < 16; ++s) sel[s] = sel4(xs[s >> 2][s & 3], mk);
    }
    const u32 tag = __builtin_amdgcn_readfirstlane(ep_raw) + 1u;
    __builtin_amdgcn_sched_barrier(0);
    f4 acc = {0.f, 0.f, 0.f, 0.f};
#pragma unroll
    for (int s = 0; s < 16; ++s) {
      acc = mfma(wv[s], sel[s], acc);
    }
    *(LAS f4*)(L + L_RED + w * 1024 + lane * 16) = acc;
#pragma unroll
    for (int s = 0; s < 4; ++s) *(LAS v4u*)(L + L_UK + w * 4096 + (4 * s + g) * 256 + ((col ^ (4 * s + g)) * 16)) = wk[s];
#pragma unroll
    for (int s = 0; s < 4; ++s) wk[s] = *(const LAS v4u*)(L + L_UK + w * 4096 + ukr * 256 + (((4 * g + s) ^ ukr) * 16));
    int qoff = WS_QA + ((lane >> 5) * 2 + (h >> 4)) * 32768 + (h & 15) * 2048 + cq * 512 + (lane & 31) * 16;
    asm volatile("" : "+v"(qoff));
    STAMP(2);
    LDS_BARRIER();
    STAMP(3);
    f4 q = *(const LAS f4*)(L + L_RED + lane * 16);
#pragma unroll
    for (int i = 1; i < 8; ++i) q += *(const LAS f4*)(L + L_RED + i * 1024 + lane * 16);
    f4 qa4 = {0.f, 0.f, 0.f, 0.f};
#pragma unroll
    for (int s = 0; s < 4; ++s) sel[s] = sel4(pk2(q[s], q[s]), mk);
#pragma unroll
    for (int s = 0; s < 4; ++s) qa4 = mfma(sel[s], wk[s], qa4);
    {
      const float t = sum32((qa4[0] + qa4[1]) + (qa4[2] + qa4[3]));
      *(LAS u16*)(L + L_QST + (g & 1) * 256 + (16 * w + col) * 2) = bf16bits(t);
    }
    LDS_BARRIER();
    if (w == 0) {
      const v2u t = *(const LAS v2u*)(L + L_QST + lane * 8);
      st_sc1(rs, qoff, v4u{t.x, tag, t.y, tag});
    }
    STAMP(4);
    LDS_BARRIER();
    u32 pre = 0;
    {
#pragma unroll
      for (int i = 0; i < 4; ++i) wu[i] = ldb<NT_ONCE>(rv, (lane >> 3) * 8192 + (lane & 7) * 16, (128 * cq + 32 * kq + 8 * i) * 8192 + h * 256 + dh * 128);
#pragma unroll
      for (int i = 0; i < 16; ++i) {
        const int row = 32 * ((cq + (i >> 2)) & 3) + 4 * w + (i & 3);
        dma<NT_SHARED>(ro, L + L_C + row * CROW, lane * 16, (h * HD + row) * 1024);
        if (i == 3) { __builtin_amdgcn_sched_barrier(0); __builtin_amdgcn_s_barrier(); __builtin_amdgcn_sched_barrier(0); }
      }
    }
    __builtin_amdgcn_sched_barrier(0);
    v4u uvf[4];
    {
      LAS unsigned char* ut = L + L_UV + w * 2048;
      LAS unsigned char* ur = ut + (4 * g + (col >> 2)) * 128 + 8 * (col & 3);
      s4 half[2][4];
#pragma unroll
      for (int r = 0; r < 2; ++r) {
        *(LAS v4u*)(ut + lane * 16) = wu[2 * r];
        *(LAS v4u*)(ut + 1024 + lane * 16) = wu[2 * r + 1];
        asm volatile("" ::: "memory");
#pragma unroll
        for (int n = 0; n < 4; ++n) half[r][n] = __builtin_amdgcn_ds_read_tr16_b64_v4i16((LAS s4*)(ur + 32 * n));
        asm volatile("" ::: "memory");
      }
#pragma unroll
      for (int n = 0; n < 4; ++n) uvf[n] = __builtin_bit_cast(v4u, __builtin_shufflevector(half[0][n], half[1][n], 0, 1, 2, 3, 4, 5, 6, 7));
    }
    DRAIN_VM();
    asm volatile("" :: "v"(pre), "v"(tch[0]), "v"(tch[1]), "v"(tch[2]), "v"(tch[3]));
    STAMP(5);
    if (w == 0) {
      const int o1[1] = {WS_SB + ((h >> 4) * 64 + lane) * 256};
      v4u t[1];
      poll<1, SSLP>(rs, o1, tag, t);
DELAY(1, DLY_B);
    }
    LDS_BARRIER();
    STAMP(6);
    {
      const int bb = (lane >> 3) & 1;
      const int offM = WS_ML + (h * 64 + bb * 32 + 16 * ((lane >> 2) & 1) + 4 * g + (lane & 3)) * 16;
      const int offB = WS_OB + (((h >> 4) * 64 + bb * 32 + 16 * (w >> 2) + 4 * g) * 16 + (h & 15)) * 2048 + (32 * cq + 8 * (w & 3) + (lane & 7)) * 16;
      v4u gv[4];
      f4 e;
#pragma unroll 1
      for (u32 spins = 0; spins < SPIN_CAP; ++spins) {
        asm volatile("" ::: "memory");
        const v4u ml = ld_sc1(rs, offM);
#pragma unroll
        for (int i = 0; i < 4; ++i) gv[i] = ld_sc1(rs, offB + i * 32768);
        if (spins == 0) {
          __builtin_amdgcn_sched_barrier(0);
#pragma unroll
          for (int t = 0; t < 4; ++t)
#pragma unroll
            for (int u = 0; u < 4; ++u) wv[t * 4 + u] = trpair(tb + (32 * u) * CROW + 32 * t, tb + (32 * u + 16) * CROW + 32 * t);
        }
        __builtin_amdgcn_sched_barrier(0);
        const float m = __uint_as_float(ml.x), l = __uint_as_float(ml.z);
        float M = fmaxf(m, dpp<XOR1>(m)); M = fmaxf(M, dpp<XOR2>(M)); M = fmaxf(M, dpp<HMIR>(M)); M = max32(max16(M));
        const float ex = __expf(m - M);
        float Ls = ex * l;
        Ls += dpp<XOR1>(Ls); Ls += dpp<XOR2>(Ls); Ls += dpp<HMIR>(Ls); Ls = sum32(sum16(Ls));
        float wn = ex / Ls;
        {
          const float up = dpp<0x114>(wn), dn = dpp<0x104>(wn);
          wn = (((lane >> 2) & 1) == (w >> 2)) ? wn : ((w >> 2) ? dn : up);
        }
        e = f4{dpp<0x00>(wn), dpp<0x55>(wn), dpp<0xAA>(wn), dpp<0xFF>(wn)};
        bool all = (ml.y == tag) & (ml.w == tag);
        __builtin_amdgcn_sched_barrier(0);
#pragma unroll
        for (int i = 0; i < 4; ++i) all = all & (gv[i].y == tag) & (gv[i].w == tag);
        if (__all(all)) break;
        __builtin_amdgcn_s_sleep(GSLP);
      }
      STAMP(7);
      float r0 = e[0] * bflo(gv[0].x), r1 = e[0] * bfhi(gv[0].x), r2 = e[0] * bflo(gv[0].z), r3 = e[0] * bfhi(gv[0].z);
#pragma unroll
      for (int i = 1; i < 4; ++i) { r0 += e[i] * bflo(gv[i].x); r1 += e[i] * bfhi(gv[i].x); r2 += e[i] * bflo(gv[i].z); r3 += e[i] * bfhi(gv[i].z); }
      const float v = swap16_add(swap32_add(r0, r2), swap32_add(r1, r3));
      *(LAS u16*)(L + L_LAT + (2 * (w >> 2) + bb) * 256 + (2 * ((w >> 1) & 1) + (g >> 1)) * 64 + (2 * (w & 1) + ((lane >> 2) & 1)) * 16 + (4 * (g & 1) + (lane & 3)) * 2) = bf16bits(v);
    }
    LDS_BARRIER();
    {
      const v4u a0 = *(const LAS v4u*)(L + L_LAT + (col & 3) * 256 + kq * 64 + g * 16);
      f4 d[4];
#pragma unroll
      for (int n = 0; n < 4; ++n) d[n] = mfma(a0, uvf[n], f4{0.f, 0.f, 0.f, 0.f});
      const f4 dl = (g & 1) ? d[1] : d[0], dm = (g & 1) ? d[3] : d[2], ds = (g & 2) ? dm : dl;
      LAS unsigned char* cx = L + L_CTX + (((2 * dh + (g >> 1)) * 8 + 2 * kq) * 4 + (col >> 2)) * 16 + (4 * (g & 1) + (col & 3)) * 2;
      *(LAS u16*)cx = bf16bits(ds[0] + ds[2]);
      *(LAS u16*)(cx + 64) = bf16bits(ds[1] + ds[3]);
    }
    LDS_BARRIER();
    STAMP(8);
    {
      v4u cf[4];
#pragma unroll
      for (int u = 0; u < 4; ++u) cf[u] = *(const LAS v4u*)(L + L_CTX + ((u * 8 + (col & 7)) * 4 + g) * 16);
      f4 o[4];
#pragma unroll
      for (int t = 0; t < 4; ++t) o[t] = f4{0.f, 0.f, 0.f, 0.f};
#pragma unroll
      for (int u = 0; u < 4; ++u)
#pragma unroll
        for (int t = 0; t < 4; ++t) o[t] = mfma(cf[u], wv[t * 4 + u], o[t]);
      float r[4];
#pragma unroll
      for (int t = 0; t < 4; ++t) r[t] = swap16_add(o[t][0] + o[t][2], o[t][1] + o[t][3]);
      if (lane < 32) st_sc1(rs, WS_SD + bid * 4096 + w * 512 + lane * 16, v4u{pk2(r[0], r[1]), tag, pk2(r[2], r[3]), tag});
    }
    STAMP(9);
    STAMP_FLUSH();
  } else {
    const int a = bid - 128, hg = (a >> 3) & 1, chunk = (a & 7) * 8 + (a >> 4), b = chunk >> 5;
    const int f0 = (NMLP * (a & 7)) >> 3, f1 = (NMLP * ((a & 7) + 1)) >> 3;
    const int fs = f0 + (a >> 3) - 8;
    const bool mlp = a >= 64 && fs < f1;
    u32 pre = 0;
    if (w == 7 && lane < (PRE_SB == 2 ? 36 : 32))
      pre = __builtin_amdgcn_raw_buffer_load_b32(rs, lane < 32 ? WS_QA + b * 65536 + ((a >> 3) * 32 + lane) * 128 : WS_SB + (b * 64 + (a >> 3) * 4 + lane - 32) * 256, 0, AUX_SC1);
    __builtin_amdgcn_s_barrier();
    __builtin_amdgcn_sched_barrier(0);
    {
      const rsrc_t rc = mkrs(p.ckv, 2 * SK * DC * 2);
#pragma unroll
      for (int i = 0; i < 16; ++i) {
        const int row = 64 * ((i >> 3) ^ hg) + 8 * w + (i & 7);
        dma<NT_SHARED>(rc, L + L_C + row * CROW, lane * 16, (chunk * 128 + row) * 1024);
        if (i == 7) { __builtin_amdgcn_sched_barrier(0); __builtin_amdgcn_s_barrier(); __builtin_amdgcn_sched_barrier(0); }
      }
    }
    const int fb = (a >> 1) & 1, fm = 64 * ((a >> 2) & 7) + 16 * ((lane >> 3) & 3) + 8 * (a & 1) + (lane & 7);
    const float fx = bflo(p.x[fb * MD + fm]), fb2 = bflo(p.b2[fm]);
    const float b1v = bflo(p.b1[32 * (mlp ? fs : 0) + (tid & 31)]);
    __builtin_amdgcn_sched_barrier(0);
    if (mlp && w >= 6) {
      float n[8];
      layer_norm8(x0, n);
      *(LAS v4u*)(L + L_XP + (w & 1) * 1024 + lane * 16) = v4u{pk2(n[0], n[1]), pk2(n[2], n[3]), pk2(n[4], n[5]), pk2(n[6], n[7])};
    }
    const u32 tag = __builtin_amdgcn_readfirstlane(ep_raw) + 1u;
    DRAIN_VM();
    asm volatile("" :: "v"(wv[0]), "v"(wv[1]), "v"(wv[2]), "v"(wv[3]), "v"(pre), "v"(tlbv));
    STAMP(1);
    int toff = (w & 2) ? MD * FF * 0 + min(((a >> 3) * 128 + (w & 1) * 64 + lane) * 128, (f1 - f0) * 32768 - 4) + f0 * 32768
                       : (lane >> 2) * (FF * 2) + min((lane & 3) * 128, (f1 - f0) * 64 - 4) + (32 * (a >> 3) + 16 * (w & 1)) * (FF * 2) + f0 * 64;
    asm volatile("" : "+v"(toff));
    const rsrc_t rt = mkrs((w & 2) ? p.w2 : p.w1, MD * FF * 2);
    LDS_BARRIER();
    STAMP(4);
    v4u cr[16];
    v4u ct[16];
    {
      v4u gv[4];
      const int off = WS_QA + (b * 2 + hg) * 32768 + w * 4096 + lane * 16;
#pragma unroll 1
      for (u32 spins = 0; spins < SPIN_CAP; ++spins) {
        asm volatile("" ::: "memory");
#pragma unroll
        for (int i = 0; i < 4; ++i) gv[i] = ld_sc1(rs, off + i * 1024);
        __builtin_amdgcn_sched_barrier(0);
        if (spins == 0) {
#pragma unroll
          for (int s = 0; s < 16; ++s) cr[s] = *(const LAS v4u*)(L + L_C + (16 * w + col) * CROW + s * 64 + g * 16);
#pragma unroll
          for (int t = 0; t < 4; ++t)
#pragma unroll
            for (int u = 0; u < 4; ++u) ct[t * 4 + u] = trpair(tb + (32 * u) * CROW + 32 * t, tb + (32 * u + 16) * CROW + 32 * t);
          STAMP(3);
        }
        __builtin_amdgcn_sched_barrier(0);
        bool all = true;
#pragma unroll
        for (int i = 0; i < 4; ++i) all = all & (gv[i].y == tag) & (gv[i].w == tag);
        if (__all(all)) break;
        __builtin_amdgcn_s_sleep(ASLP);
      }
#pragma unroll
      for (int i = 0; i < 4; ++i) *(LAS v2u*)(L + L_Q + (2 * w + (i >> 1)) * CROW + (i & 1) * 512 + lane * 8) = v2u{gv[i].x, gv[i].z};
    }
    v4u w1v[4], w2v[4];
    const rsrc_t r1 = mkrs(p.w1, MD * FF * 2), r2 = mkrs(p.w2, FF * MD * 2);
    LAS unsigned char* qrd = L + L_Q + col * CROW + g * 16;
    asm volatile("" : "+v"(qrd));
    LDS_BARRIER();
    STAMP(5);
    f4 sc = {0.f, 0.f, 0.f, 0.f};
    u32 touch = 0;
    {
      v4u qf[16];
#pragma unroll
      for (int s = 0; s < 16; ++s) {
        qf[s] = *(const LAS v4u*)(qrd + s * 64);
        if (s == 3) {
          __builtin_amdgcn_sched_barrier(0);
          if (w >= 4) touch = __builtin_amdgcn_raw_buffer_load_b32(rt, toff, 0, 0);
          __builtin_amdgcn_sched_barrier(0);
        }
      }
      __builtin_amdgcn_sched_barrier(0);
#pragma unroll
      for (int s = 0; s < 16; ++s) sc = mfma(cr[s], qf[s], sc);
    }

#pragma unroll
    for (int r = 0; r < 4; ++r) sc[r] = rbf(rbf(sc[r]) * 0.08838834764831845f);
    float mx = fmaxf(fmaxf(sc[0], sc[1]), fmaxf(sc[2], sc[3]));
    mx = max32(max16(mx));
    if (lane < 16) *(LAS float*)(L + L_MX + lane * 32 + w * 4) = mx;
    if (tid == 0) st_sc1(rs, WS_SB + (hg * 64 + chunk) * 256, v4u{0u, tag, 0u, tag});
    LDS_BARRIER();
    {
      const f4 m0 = *(const LAS f4*)(L + L_MX + col * 32), m1 = *(const LAS f4*)(L + L_MX + col * 32 + 16);
      mx = fmaxf(fmaxf(fmaxf(m0[0], m0[1]), fmaxf(m0[2], m0[3])), fmaxf(fmaxf(m1[0], m1[1]), fmaxf(m1[2], m1[3])));
    }
    {
      const float p0 = __expf(sc[0] - mx), p1 = __expf(sc[1] - mx), p2 = __expf(sc[2] - mx), p3 = __expf(sc[3] - mx);
      *(LAS v2u*)(L + L_P + (w >> 1) * 1024 + lane * 16 + (w & 1) * 8) = v2u{pk2(p0, p1), pk2(p2, p3)};
    }
    LDS_BARRIER();
    STAMP(6);
    v4u pf[4];
#pragma unroll
    for (int u = 0; u < 4; ++u) pf[u] = *(const LAS v4u*)(L + L_P + u * 1024 + lane * 16);
    if (w == 7) {
      float l = 0.f;
      const b2 one = {(__bf16)1.0f, (__bf16)1.0f};
#pragma unroll
      for (int u = 0; u < 4; ++u)
#pragma unroll
        for (int i = 0; i < 4; ++i) { const u32 pp = pf[u][i]; l = __builtin_amdgcn_fdot2_f32_bf16(__builtin_bit_cast(b2, pp), one, l, false); }
      l = sum32(sum16(l));
      if (lane < 16) st_sc1(rs, WS_ML + ((16 * hg + lane) * 64 + chunk) * 16, v4u{__float_as_uint(mx), tag, __float_as_uint(l), tag});
    }
    f4 o[4];
#pragma unroll
    for (int t = 0; t < 4; ++t) o[t] = f4{0.f, 0.f, 0.f, 0.f};
#pragma unroll
    for (int u = 0; u < 4; ++u)
#pragma unroll
      for (int t = 0; t < 4; ++t) o[t] = mfma(pf[u], ct[t * 4 + u], o[t]);
    {
      const int base = WS_OB + ((hg * 64 + chunk) * 16 + 4 * g) * 2048 + (16 * w + col) * 16;
#pragma unroll
      for (int r = 0; r < 4; ++r) st_sc1(rs, base + r * 2048, v4u{pk2(o[0][r], o[1][r]), tag, pk2(o[2][r], o[3][r]), tag});
    }
    STAMP(7);
    __builtin_amdgcn_sched_barrier(0);
    asm volatile("" :: "v"(touch));
    if (mlp) {
      __builtin_amdgcn_s_barrier();
#pragma unroll
      for (int i = 0; i < 4; ++i) w1v[i] = ldb<NT_ONCE>(r1, (lane >> 2) * (FF * 2) + (lane & 3) * 16, (64 * w + 16 * i) * (FF * 2) + fs * 64);
#pragma unroll
      for (int i = 0; i < 4; ++i) w2v[i] = ldb<NT_ONCE>(r2, lane * 16, (32 * fs + 4 * w + i) * 1024);
      __builtin_amdgcn_sched_barrier(0);
      LAS unsigned char* wt = L + L_W1 + w * 4096;
#pragma unroll
      for (int i = 0; i < 4; ++i) {
        *(LAS v4u*)(wt + i * 1024 + lane * 16) = w1v[i];
        *(LAS v4u*)(L + L_C + (4 * w + i) * CROW + lane * 16) = w2v[i];
      }
      asm volatile("" ::: "memory");
      f4 h0 = {0.f, 0.f, 0.f, 0.f}, h1 = {0.f, 0.f, 0.f, 0.f};
#pragma unroll
      for (int u = 0; u < 2; ++u) {
        const v4u xf = *(const LAS v4u*)(L + L_XP + (col & 1) * 1024 + w * 128 + u * 64 + g * 16);
        LAS unsigned char* ur = wt + (32 * u + 8 * g + (col >> 2)) * 64 + 8 * (col & 3);
        h0 = mfma(xf, trpair(ur, ur + 256), h0);
        h1 = mfma(xf, trpair(ur + 32, ur + 288), h1);
      }
      {
        const float lo = (g & 2) ? h0[1] : h0[0], hi = (g & 2) ? h1[1] : h1[0];
        *(LAS float*)(L + L_HR + w * 256 + lane * 4) = (g & 1) ? hi : lo;
      }
      LDS_BARRIER();
      if (tid < 64) {
        float s = 0.f;
#pragma unroll
        for (int i = 0; i < 8; ++i) s += *(const LAS float*)(L + L_HR + i * 256 + tid * 4);
        const float hv = rbf(rbf(s) + b1v);
        const float u = 0.7978845608028654f * (hv + 0.044715f * hv * hv * hv);
        const float ge = hv / (1.0f + __expf(-2.0f * u));
        *(LAS u16*)(L + L_HX + ((g >> 1) * 4 + (col >> 2)) * 16 + (4 * (g & 1) + (col & 3)) * 2) = bf16bits(ge);
      }
      LDS_BARRIER();
      const v4u hf = *(const LAS v4u*)(L + L_HX + ((col & 1) * 4 + g) * 16);
      float r[4];
#pragma unroll
      for (int t = 0; t < 4; ++t) {
        const f4 y = mfma(hf, trpair(tb + 32 * t, tb + 16 * CROW + 32 * t), f4{0.f, 0.f, 0.f, 0.f});
        r[t] = (g & 1) ? y[1] : y[0];
      }
      if (lane < 32) st_sc1(rs, WS_SD + (128 + fs) * 4096 + w * 512 + lane * 16, v4u{pk2(r[0], r[1]), tag, pk2(r[2], r[3]), tag});
      STAMP(8);
    }
    if (a < 32) {
      v4u gv[3];
      int off[3];
      const int slot = tid >> 3;
      const bool hasy = slot < NMLP;
      off[0] = WS_SD + slot * 4096 + a * 128 + (lane & 7) * 16;
      off[1] = off[0] + 64 * 4096;
      off[2] = WS_SD + (128 + (hasy ? slot : 0)) * 4096 + a * 128 + (lane & 7) * 16;
      poll<3, DSLP>(rs, off, tag, gv);
      STAMP(9);
      if (!hasy) { gv[2].x = 0u; gv[2].z = 0u; }
      const float t0 = swap32_add(bflo(gv[0].x) + bflo(gv[1].x), bflo(gv[2].x)), t1 = swap32_add(bfhi(gv[0].x) + bfhi(gv[1].x), bfhi(gv[2].x));
      const float t2 = swap32_add(bflo(gv[0].z) + bflo(gv[1].z), bflo(gv[2].z)), t3 = swap32_add(bfhi(gv[0].z) + bfhi(gv[1].z), bfhi(gv[2].z));
      float u0 = swap16_add(t0, t1), u1 = swap16_add(t2, t3);
      u0 += dpp<ROR8>(u0); u1 += dpp<ROR8>(u1);
      if ((lane & 8) == 0) *(LAS f2*)(L + L_FIN + w * 256 + g * 64 + (lane & 7) * 8) = f2{u0, u1};
      LDS_BARRIER();
      if (a == 0 && tid == 64) *(GAS u32*)(p.ws + WS_EPOCH) = tag;
      if (tid < 32) {
        float s = 0.f, y = 0.f;
        const int o = ((lane >> 3) & 1) * 64 + (lane & 7) * 8 + (lane >> 4) * 4;
#pragma unroll
        for (int i = 0; i < 8; ++i) {
          s += *(const LAS float*)(L + L_FIN + i * 256 + o);
          y += *(const LAS float*)(L + L_FIN + i * 256 + 128 + o);
        }
        s = rbf(s);
        y = rbf(rbf(y) + fb2);
        const float gt = rbf(y / (1.0f + __expf(-y)));
        const float r = fx + rbf(s * gt);
        p.out[fb * MD + fm] = bf16bits(r);
      }
      STAMP(10);
    }
    STAMP_FLUSH();
  }
}

extern "C" void kernel_launch(void* const* d_in, const int* in_sizes, int n_in, void* d_out, int out_size, void* d_ws, size_t ws_size,
                              hipStream_t stream) {
  Params p;
  p.x = (const u16*)d_in[0];
  p.ckv = (const u16*)d_in[1];
  p.wq = (const u16*)d_in[2];
  p.wuk = (const u16*)d_in[3];
  p.wuv = (const u16*)d_in[4];
  p.wo = (const u16*)d_in[5];
  p.w1 = (const u16*)d_in[7];
  p.b1 = (const u16*)d_in[8];
  p.w2 = (const u16*)d_in[9];
  p.b2 = (const u16*)d_in[10];
  p.out = (u16*)d_out;
  p.ws = (unsigned char*)d_ws;
  (void)hipFuncSetAttribute((const void*)mla_decode_kernel, hipFuncAttributeMaxDynamicSharedMemorySize, LDS_BYTES);
  hipLaunchKernelGGL(mla_decode_kernel, dim3(256), dim3(512), LDS_BYTES, stream, p);
}
```
